```python
import torch

def _softmax(x, axis=-1):
    return torch.softmax(x, dim=axis)
def _gelu(x):
    return torch.nn.functional.gelu(x, approximate="tanh")
def _silu(x):
    return torch.nn.functional.silu(x)
def _sigmoid(x):
    return torch.sigmoid(x)
def _layernorm(x, axis=-1, eps=1e-5):
    m = x.mean(dim=axis, keepdim=True)
    v = x.var(dim=axis, keepdim=True, unbiased=False)
    return (x - m) / torch.sqrt(v + eps)
def _scatter_add(tgt, idx, src, axis=0):
    return tgt.clone().index_add_(axis, idx, src)
def _segment_sum(src, seg, n):
    out = torch.zeros((n,) + src.shape[1:], dtype=src.dtype)
    return out.index_add_(0, seg, src)
def _pad(x, pw):
    flat = tuple(v for pair in reversed(pw) for v in pair)
    return torch.nn.functional.pad(x, flat)

kernel_name = 'interleave_layers__grouped_kv_mqa__sliding_block__prompt'

def setup_inputs():

    _w = (lambda a: (lambda t: t.to(torch.bfloat16) if t.is_floating_point() else t)(torch.as_tensor(a)))
    def _A_setup():
        B, S, D, H = 1, 2048, 640, 32
        Dh = D // H
        return {
            "x": _w((lambda *s: torch.randn(*s, dtype=torch.bfloat16) * 0.05)(B, S, D)),
            "wq": _w((lambda *s: torch.randn(*s, dtype=torch.bfloat16) * 0.05)(D, H * Dh)),
            "wk": _w((lambda *s: torch.randn(*s, dtype=torch.bfloat16) * 0.05)(D, Dh)),
            "wv": _w((lambda *s: torch.randn(*s, dtype=torch.bfloat16) * 0.05)(D, Dh)),
            "wo": _w((lambda *s: torch.randn(*s, dtype=torch.bfloat16) * 0.05)(H * Dh, D)),
        }
    _A_in = _A_setup()

    def _B_setup():
        B, S, D = 1, 2048, 640
        return {
            "x": _w((lambda *s: torch.randn(*s, dtype=torch.bfloat16) * 0.05)(B, S, D)),
            "wqkv": _w((lambda *s: torch.randn(*s, dtype=torch.bfloat16) * 0.05)(D, 3 * D)),
            "wo": _w((lambda *s: torch.randn(*s, dtype=torch.bfloat16) * 0.05)(D, D)),
        }
    _B_in = _B_setup()

    return {
        "A_x": _A_in["x"],
        "A_wq": _A_in["wq"],
        "A_wk": _A_in["wk"],
        "A_wv": _A_in["wv"],
        "A_wo": _A_in["wo"],
        "B_x": _B_in["x"],
        "B_wqkv": _B_in["wqkv"],
        "B_wo": _B_in["wo"],
    }

def reference(A_x, A_wq, A_wk, A_wv, A_wo, B_x, B_wqkv, B_wo):
    def _A_ref(x, wq, wk, wv, wo):
        B, S, D = x.shape
        H = 32
        Dh = D // H
        q = (lambda x, *d: x.permute(*d))((x @ wq).reshape(B, S, H, Dh), 0, 2, 1, 3)
        k = x @ wk
        v = x @ wv
        scores = torch.einsum("bhqd,bkd->bhqk", q, k) * (Dh ** -0.5)
        p = _softmax(scores, axis=-1)
        o = torch.einsum("bhqk,bkd->bhqd", p, v)
        return (lambda x, *d: x.permute(*d))(o, 0, 2, 1, 3).reshape(B, S, H * Dh) @ wo

    def _B_ref(x, wqkv, wo):
        B, S, D = x.shape
        H = 32; Dh = D // H; W = 256
        qkv = (lambda x, *d: x.permute(*d))((x @ wqkv).reshape(B, S, 3, H, Dh), 2, 0, 3, 1, 4)
        q, k, v = qkv[0], qkv[1], qkv[2]
        scores = torch.einsum("bhqd,bhkd->bhqk", q, k) * (Dh ** -0.5)
        idx = torch.arange(S)
        mask = (idx[:, None] - idx[None, :] >= 0) & (idx[:, None] - idx[None, :] < W)
        scores = torch.where(mask[None, None], scores, -1e9)
        p = _softmax(scores, axis=-1)
        o = torch.einsum("bhqk,bhkd->bhqd", p, v)
        return (lambda x, *d: x.permute(*d))(o, 0, 2, 1, 3).reshape(B, S, D) @ wo

    x = A_x
    x = x + _A_ref(_layernorm(x, axis=-1), A_wq, A_wk, A_wv, A_wo)
    x = x + _B_ref(_layernorm(x, axis=-1), B_wqkv, B_wo)
    x = x + _A_ref(_layernorm(x, axis=-1), A_wq, A_wk, A_wv, A_wo)
    x = x + _B_ref(_layernorm(x, axis=-1), B_wqkv, B_wo)
    return x
```

```cpp
#include <hip/hip_runtime.h>
#include <hip/hip_bf16.h>
#include <stdint.h>

typedef __attribute__((ext_vector_type(8))) short bf16x8;
typedef __attribute__((ext_vector_type(4))) short s16x4;
typedef __attribute__((ext_vector_type(16))) float f32x16;
typedef __attribute__((ext_vector_type(4))) unsigned u32x4;
typedef __attribute__((ext_vector_type(2))) unsigned u32x2;
typedef unsigned short u16;

constexpr int S = 2048, D = 640, H = 32;
constexpr float QSCALE = 0.22360679774997896f * 1.4426950408889634f;
constexpr float THR = 10.0f;

__device__ __forceinline__ unsigned cvtpk(float lo, float hi) {
  unsigned r; asm volatile("v_cvt_pk_bf16_f32 %0, %1, %2" : "=v"(r) : "v"(lo), "v"(hi)); return r;
}
__device__ __forceinline__ float bf2f(unsigned v16) { return __uint_as_float(v16 << 16); }

constexpr int XSTR = 1296;
constexpr int KC = 64;

template <int MODE>
__global__ __launch_bounds__(512) void gemm_k(const u16* __restrict__ X, const u16* __restrict__ W0,
                                              const u16* __restrict__ W1, const u16* __restrict__ W2,
                                              const u16* __restrict__ R, u16* __restrict__ O0,
                                              u16* __restrict__ O1) {
  constexpr int NBW = (MODE == 1) ? 2 : 1;
  constexpr int BN = 128 * NBW;
  constexpr int WSTR = BN * 2 + 64;
  constexpr int WBUF = KC * WSTR;
  constexpr int NPASS = (KC * BN * 2) / (512 * 16);
  extern __shared__ __attribute__((aligned(16))) char smem[];
  char* xs = smem;
  char* ws = smem + 64 * XSTR;

  const int tid = threadIdx.x, wid = tid >> 6, lane = tid & 63;
  const int m0 = blockIdx.x * 64, n0 = blockIdx.y * BN;

  u32x4 wr[NPASS];
  auto wload = [&](int c) {
#pragma unroll
    for (int p = 0; p < NPASS; ++p) {
      int idx = p * 512 + tid;
      int k = c * KC + idx / (BN / 8), n = n0 + (idx % (BN / 8)) * 8;
      if (MODE == 0) {
        if (n < 640) wr[p] = *(const u32x4*)(W0 + k * 640 + n);
        else {
          unsigned e[8];
#pragma unroll
          for (int j = 0; j < 8; ++j) { int nn = n + j;
            e[j] = nn < 660 ? W1[k * 20 + nn - 640] : (nn < 680 ? W2[k * 20 + nn - 660] : 0); }
          wr[p] = u32x4{e[0] | (e[1] << 16), e[2] | (e[3] << 16), e[4] | (e[5] << 16), e[6] | (e[7] << 16)};
        }
      } else if (MODE == 1) {
        wr[p] = n < 1920 ? *(const u32x4*)(W0 + k * 1920 + n) : u32x4{0, 0, 0, 0};
      } else {
        wr[p] = *(const u32x4*)(W0 + k * 640 + n);
      }
    }
  };
  auto wstore = [&](int b) {
#pragma unroll
    for (int p = 0; p < NPASS; ++p) {
      int idx = p * 512 + tid;
      *(u32x4*)(ws + b * WBUF + (idx / (BN / 8)) * WSTR + (idx % (BN / 8)) * 16) = wr[p];
    }
  };
  wload(0);

  {
    const int sub = tid & 15;
#pragma unroll
    for (int pass = 0; pass < 2; ++pass) {
      const int row = pass * 32 + (tid >> 4);
      const u16* src = X + (size_t)(m0 + row) * 640;
      u32x4 v[5];
#pragma unroll
      for (int i = 0; i < 5; ++i) v[i] = *(const u32x4*)(src + (sub + 16 * i) * 8);
      if (MODE != 2) {
        float f[40]; float s = 0;
#pragma unroll
        for (int i = 0; i < 5; ++i)
#pragma unroll
          for (int j = 0; j < 4; ++j) { f[i * 8 + 2 * j] = bf2f(v[i][j] & 0xffff); f[i * 8 + 2 * j + 1] = __uint_as_float(v[i][j] & 0xffff0000u); }
#pragma unroll
        for (int i = 0; i < 40; ++i) s += f[i];
#pragma unroll
        for (int o = 1; o < 16; o <<= 1) s += __shfl_xor(s, o);
        const float mean = s * (1.0f / 640.0f);
        float q = 0;
#pragma unroll
        for (int i = 0; i < 40; ++i) { f[i] -= mean; q += f[i] * f[i]; }
#pragma unroll
        for (int o = 1; o < 16; o <<= 1) q += __shfl_xor(q, o);
        const float rs = 1.0f / sqrtf(q * (1.0f / 640.0f) + 1e-5f);
#pragma unroll
        for (int i = 0; i < 5; ++i)
#pragma unroll
          for (int j = 0; j < 4; ++j) v[i][j] = cvtpk(f[i * 8 + 2 * j] * rs, f[i * 8 + 2 * j + 1] * rs);
      }
#pragma unroll
      for (int i = 0; i < 5; ++i) *(u32x4*)(xs + row * XSTR + (sub + 16 * i) * 16) = v[i];
    }
  }
  wstore(0);
  __syncthreads();

  const int mb = wid & 1, nbw = (wid >> 1) * NBW;
  const int r32 = lane & 31, hi = lane >> 5;
  f32x16 acc[NBW];
#pragma unroll
  for (int i = 0; i < NBW; ++i) acc[i] = f32x16{};
  const char* xrow = xs + (mb * 32 + r32) * XSTR + hi * 16;
  const int trq = (lane >> 2) & 3, trp = lane & 3, trg = (lane >> 4) & 1;
  const int troff = (8 * hi + trq) * WSTR + (nbw * 32 + 16 * trg + 4 * trp) * 2;

  for (int c = 0; c < 640 / KC; ++c) {
    if (c + 1 < 640 / KC) wload(c + 1);
    const char* wb = ws + (c & 1) * WBUF + troff;
#pragma unroll
    for (int ks = 0; ks < KC / 16; ++ks) {
      bf16x8 xf = *(const bf16x8*)(xrow + (c * KC + ks * 16) * 2);
#pragma unroll
      for (int i = 0; i < NBW; ++i) {
        const char* p = wb + ks * 16 * WSTR + i * 64;
        s16x4 a = __builtin_amdgcn_ds_read_tr16_b64_v4i16((__attribute__((address_space(3))) s16x4*)(p));
        s16x4 b = __builtin_amdgcn_ds_read_tr16_b64_v4i16((__attribute__((address_space(3))) s16x4*)(p + 4 * WSTR));
        bf16x8 wf = bf16x8{a[0], a[1], a[2], a[3], b[0], b[1], b[2], b[3]};
        acc[i] = __builtin_amdgcn_mfma_f32_32x32x16_bf16(wf, xf, acc[i], 0, 0, 0);
      }
    }
    if (c + 1 < 640 / KC) wstore((c + 1) & 1);
    __syncthreads();
  }

  const int tok = m0 + mb * 32 + r32;
#pragma unroll
  for (int i = 0; i < NBW; ++i) {
#pragma unroll
    for (int g = 0; g < 4; ++g) {
      const int n = n0 + (nbw + i) * 32 + 8 * g + 4 * hi;
      float a0 = acc[i][4 * g], a1 = acc[i][4 * g + 1], a2 = acc[i][4 * g + 2], a3 = acc[i][4 * g + 3];
      if (MODE == 2) {
        u32x2 rv = *(const u32x2*)(R + (size_t)tok * 640 + n);
        a0 += bf2f(rv[0] & 0xffff); a1 += __uint_as_float(rv[0] & 0xffff0000u);
        a2 += bf2f(rv[1] & 0xffff); a3 += __uint_as_float(rv[1] & 0xffff0000u);
        *(u32x2*)(O0 + (size_t)tok * 640 + n) = u32x2{cvtpk(a0, a1), cvtpk(a2, a3)};
      } else {
        const int lim = MODE == 0 ? 680 : 1920;
        if (n >= lim) continue;
        const int T = tok >> 5, r = tok & 31;
        if (n < 640) {
          const int hd = n / 20, d = n - hd * 20;
          u16* q = O0 + (size_t)tok * 1024 + hd * 32 + d;
          *(u32x2*)q = u32x2{cvtpk(a0 * QSCALE, a1 * QSCALE), cvtpk(a2 * QSCALE, a3 * QSCALE)};
          if (d == 16) { *(u32x2*)(q + 4) = u32x2{0, 0}; *(u32x4*)(q + 8) = u32x4{0, 0, 0, 0}; }
        } else {
          const int kvw = MODE == 0 ? 20 : 640;
          const bool isv = n >= 640 + kvw;
          const int nn = n - 640 - (isv ? kvw : 0);
          const int hd = nn / 20, d = nn - hd * 20;
          u16* base = O1 + ((size_t)hd * 64 + T) * 2048;
          if (!isv) {
            u16* kp = base + (d >> 4) * 512 + (((d >> 3) & 1) * 32 + r) * 8 + (d & 7);
            *(u32x2*)kp = u32x2{cvtpk(a0, a1), cvtpk(a2, a3)};
            if (d == 16) { *(u32x2*)(kp + 4) = u32x2{0, 0}; *(u32x4*)(kp + 256) = u32x4{0, 0, 0, 0}; }
          } else {
            const int u = r & 15, sgrp = r >> 4;
            u16* vp = base + (2 + sgrp) * 512 + (((u >> 2) & 1) * 32 + d) * 8 + (u >> 3) * 4 + (u & 3);
            unsigned p0 = cvtpk(a0, a1), p1 = cvtpk(a2, a3);
            vp[0] = p0 & 0xffff; vp[8] = p0 >> 16; vp[16] = p1 & 0xffff; vp[24] = p1 >> 16;
            if (d == 16) vp[32] = 0x3f80;
          }
        }
      }
    }
  }
}

struct AttnState { f32x16 o; float negm; };

template <bool FIRST>
__device__ __forceinline__ void attn_tile(AttnState& st, f32x16 s, bf16x8 v0, bf16x8 v1) {
  float cm = fmaxf(fmaxf(s[0], s[1]), s[2]);
#pragma unroll
  for (int i = 3; i < 15; i += 2) cm = fmaxf(fmaxf(cm, s[i]), s[i + 1]);
  cm = fmaxf(cm, s[15]);
  cm = fmaxf(cm, __shfl_xor(cm, 32));
  if (FIRST) {
    st.negm -= cm;
#pragma unroll
    for (int i = 0; i < 16; ++i) s[i] -= cm;
  } else if (__any(cm > THR)) {
    const float d = fmaxf(cm, 0.f);
    const float al = __builtin_amdgcn_exp2f(-d);
    st.negm -= d;
#pragma unroll
    for (int i = 0; i < 16; ++i) { s[i] -= d; st.o[i] *= al; }
  }
#pragma unroll
  for (int i = 0; i < 16; ++i) s[i] = __builtin_amdgcn_exp2f(s[i]);
  u32x4 p0 = {cvtpk(s[0], s[1]), cvtpk(s[2], s[3]), cvtpk(s[4], s[5]), cvtpk(s[6], s[7])};
  u32x4 p1 = {cvtpk(s[8], s[9]), cvtpk(s[10], s[11]), cvtpk(s[12], s[13]), cvtpk(s[14], s[15])};
  st.o = __builtin_amdgcn_mfma_f32_32x32x16_bf16(v0, *(bf16x8*)&p0, st.o, 0, 0, 0);
  st.o = __builtin_amdgcn_mfma_f32_32x32x16_bf16(v1, *(bf16x8*)&p1, st.o, 0, 0, 0);
}
__device__ __forceinline__ f32x16 qk(bf16x8 k0, bf16x8 k1, bf16x8 q0, bf16x8 q1, float negm) {
  f32x16 c;
#pragma unroll
  for (int i = 0; i < 16; ++i) c[i] = negm;
  c = __builtin_amdgcn_mfma_f32_32x32x16_bf16(k0, q0, c, 0, 0, 0);
  return __builtin_amdgcn_mfma_f32_32x32x16_bf16(k1, q1, c, 0, 0, 0);
}
__device__ __forceinline__ void attn_store(const AttnState& st, u16* dst  , int lane) {
  const int hi = lane >> 5;
  const float l = __shfl(st.o[8], (lane & 31) + 32);
  const float inv = 1.0f / l;
#pragma unroll
  for (int g = 0; g < 3; ++g) {
    if (g == 2 && hi) break;
    *(u32x2*)(dst + 8 * g + 4 * hi) = u32x2{cvtpk(st.o[4 * g] * inv, st.o[4 * g + 1] * inv), cvtpk(st.o[4 * g + 2] * inv, st.o[4 * g + 3] * inv)};
  }
}

constexpr int CH = 4;
__global__ __launch_bounds__(512) void attnA_k(const u16* __restrict__ Qp, const u16* __restrict__ KV, u16* __restrict__ O) {
  __shared__ __attribute__((aligned(16))) char lds[2 * CH * 4096];
  const int tid = threadIdx.x, wid = tid >> 6, lane = tid & 63;
  const int tok = blockIdx.x * 8 + wid, c = lane & 31, hi = lane >> 5;
  const u16* qp = Qp + (size_t)tok * 1024 + c * 32 + hi * 8;
  const bf16x8 q0 = *(const bf16x8*)qp, q1 = *(const bf16x8*)(qp + 16);
  AttnState st; st.o = f32x16{}; st.negm = 0.f;
  u32x4 sr[2];
  const u32x4* src = (const u32x4*)KV + tid;
  sr[0] = src[0]; sr[1] = src[512];
  *(u32x4*)(lds + tid * 16) = sr[0]; *(u32x4*)(lds + 8192 + tid * 16) = sr[1];
  __syncthreads();
  constexpr int NCH = 64 / CH;
  for (int ch = 0; ch < NCH; ++ch) {
    if (ch + 1 < NCH) { sr[0] = src[(ch + 1) * 1024]; sr[1] = src[(ch + 1) * 1024 + 512]; }
    const char* b = lds + (ch & 1) * (CH * 4096) + lane * 16;
#pragma unroll
    for (int t = 0; t < CH; ++t) {
      const bf16x8 k0 = *(const bf16x8*)(b + t * 4096), k1 = *(const bf16x8*)(b + t * 4096 + 1024);
      const bf16x8 v0 = *(const bf16x8*)(b + t * 4096 + 2048), v1 = *(const bf16x8*)(b + t * 4096 + 3072);
      f32x16 s = qk(k0, k1, q0, q1, st.negm);
      if (t == 0 && ch == 0) attn_tile<true>(st, s, v0, v1); else attn_tile<false>(st, s, v0, v1);
    }
    if (ch + 1 < NCH) { char* d = lds + ((ch + 1) & 1) * (CH * 4096) + tid * 16; *(u32x4*)d = sr[0]; *(u32x4*)(d + 8192) = sr[1]; }
    __syncthreads();
  }
  attn_store(st, O + (size_t)tok * 640 + c * 20, lane);
}

__global__ __launch_bounds__(512) void attnB_k(const u16* __restrict__ Qp, const u16* __restrict__ KV, u16* __restrict__ O) {
  const int tid = threadIdx.x, wid = tid >> 6, lane = tid & 63;
  const int gw = blockIdx.x * 8 + wid, head = gw & 31, qt = gw >> 5;
  const int c = lane & 31, hi = lane >> 5, tok = qt * 32 + c;
  const u16* qp = Qp + (size_t)tok * 1024 + head * 32 + hi * 8;
  const bf16x8 q0 = *(const bf16x8*)qp, q1 = *(const bf16x8*)(qp + 16);
  AttnState st; st.o = f32x16{}; st.negm = 0.f;
  const u16* kvb = KV + (size_t)head * 64 * 2048 + lane * 8;
  const int tlo = qt >= 8 ? qt - 8 : 0;
  for (int T = qt; T >= tlo; --T) {
    const u16* b = kvb + T * 2048;
    const bf16x8 k0 = *(const bf16x8*)b, k1 = *(const bf16x8*)(b + 512);
    const bf16x8 v0 = *(const bf16x8*)(b + 1024), v1 = *(const bf16x8*)(b + 1536);
    f32x16 s = qk(k0, k1, q0, q1, st.negm);
    if (T == qt) {
#pragma unroll
      for (int i = 0; i < 16; ++i) { const int row = (i & 3) + 8 * (i >> 2) + 4 * hi; if (row > c) s[i] = -1e30f; }
      attn_tile<true>(st, s, v0, v1);
    } else {
      if (T == qt - 8) {
#pragma unroll
        for (int i = 0; i < 16; ++i) { const int row = (i & 3) + 8 * (i >> 2) + 4 * hi; if (row <= c) s[i] = -1e30f; }
      }
      attn_tile<false>(st, s, v0, v1);
    }
  }
  attn_store(st, O + (size_t)tok * 640 + head * 20, lane);
}

extern "C" void kernel_launch(void* const* d_in, const int* in_sizes, int n_in, void* d_out, int out_size,
                              void* d_ws, size_t ws_size, hipStream_t stream) {
  const u16* x = (const u16*)d_in[0];
  const u16 *wq = (const u16*)d_in[1], *wk = (const u16*)d_in[2], *wv = (const u16*)d_in[3], *woA = (const u16*)d_in[4];
  const u16 *wqkv = (const u16*)d_in[6], *woB = (const u16*)d_in[7];
  char* w = (char*)d_ws;
  u16* xb0 = (u16*)(w);
  u16* xb1 = (u16*)(w + (4u << 20));
  u16* Qp = (u16*)(w + (8u << 20));
  u16* KV = (u16*)(w + (16u << 20));
  u16* ob = (u16*)(w + (28u << 20));
  const size_t lds1 = 64 * XSTR + 2 * KC * (128 * 2 + 64), lds2 = 64 * XSTR + 2 * KC * (256 * 2 + 64);
  hipFuncSetAttribute((const void*)gemm_k<0>, hipFuncAttributeMaxDynamicSharedMemorySize, (int)lds1);
  hipFuncSetAttribute((const void*)gemm_k<1>, hipFuncAttributeMaxDynamicSharedMemorySize, (int)lds2);
  hipFuncSetAttribute((const void*)gemm_k<2>, hipFuncAttributeMaxDynamicSharedMemorySize, (int)lds1);
  const u16* xin = x;
  u16* outs[4] = {xb0, xb1, xb0, (u16*)d_out};
  for (int layer = 0; layer < 4; ++layer) {
    if ((layer & 1) == 0) {
      gemm_k<0><<<dim3(32, 6), 512, lds1, stream>>>(xin, wq, wk, wv, nullptr, Qp, KV);
      attnA_k<<<256, 512, 0, stream>>>(Qp, KV, ob);
      gemm_k<2><<<dim3(32, 5), 512, lds1, stream>>>(ob, woA, nullptr, nullptr, xin, outs[layer], nullptr);
    } else {
      gemm_k<1><<<dim3(32, 8), 512, lds2, stream>>>(xin, wqkv, nullptr, nullptr, nullptr, Qp, KV);
      attnB_k<<<256, 512, 0, stream>>>(Qp, KV, ob);
      gemm_k<2><<<dim3(32, 5), 512, lds1, stream>>>(ob, woB, nullptr, nullptr, xin, outs[layer], nullptr);
    }
    xin = outs[layer];
  }
}
```

```cpp
#include <hip/hip_runtime.h>
#include <hip/hip_bf16.h>
#include <stdint.h>

typedef __attribute__((ext_vector_type(8))) short bf16x8;
typedef __attribute__((ext_vector_type(4))) short s16x4;
typedef __attribute__((ext_vector_type(16))) float f32x16;
typedef __attribute__((ext_vector_type(4))) unsigned u32x4;
typedef __attribute__((ext_vector_type(2))) unsigned u32x2;
typedef unsigned short u16;

constexpr int S = 2048, D = 640, H = 32;
constexpr float QSCALE = 0.22360679774997896f * 1.4426950408889634f;
constexpr float THR = 10.0f;

__device__ __forceinline__ unsigned cvtpk(float lo, float hi) {
  unsigned r; asm volatile("v_cvt_pk_bf16_f32 %0, %1, %2" : "=v"(r) : "v"(lo), "v"(hi)); return r;
}
__device__ __forceinline__ float bf2f(unsigned v16) { return __uint_as_float(v16 << 16); }

__global__ __launch_bounds__(256) void prep_k(const u16* __restrict__ wq, const u16* __restrict__ wk, const u16* __restrict__ wv,
                                              const u16* __restrict__ woA, const u16* __restrict__ wqkv, const u16* __restrict__ woB,
                                              u16* __restrict__ dst) {
  const int g = blockIdx.x * 256 + threadIdx.x;
  const int lane = g & 63, fr = g >> 6, nbg = fr / 40, ks = fr - nbg * 40;
  const int r = lane & 31, h = lane >> 5;
  unsigned e[8];
  const u16* src; int ld, n;
  if (nbg < 22) { n = nbg * 32 + r; src = wq; ld = 640;
    if (n >= 640) { if (n < 660) { src = wk; ld = 20; n -= 640; } else if (n < 680) { src = wv; ld = 20; n -= 660; } else src = nullptr; } }
  else if (nbg < 42) { n = (nbg - 22) * 32 + r; src = woA; ld = 640; }
  else if (nbg < 102) { n = (nbg - 42) * 32 + r; src = wqkv; ld = 1920; }
  else { n = (nbg - 102) * 32 + r; src = woB; ld = 640; }
#pragma unroll
  for (int j = 0; j < 8; ++j) e[j] = src ? src[(16 * ks + 8 * h + j) * ld + n] : 0;
  *(u32x4*)(dst + (size_t)g * 8) = u32x4{e[0] | (e[1] << 16), e[2] | (e[3] << 16), e[4] | (e[5] << 16), e[6] | (e[7] << 16)};
}

template <int MODE>
__global__ __launch_bounds__(256, 2) void gemm_k(const u16* __restrict__ X, const u16* __restrict__ WT,
                                              const u16* __restrict__ R, u16* __restrict__ O0,
                                              u16* __restrict__ O1, int nblk) {
  __shared__ __attribute__((aligned(16))) char xs[64 * 1280];
  const int tid = threadIdx.x, wid = tid >> 6, lane = tid & 63;
  const int m0 = blockIdx.x * 64, nb = blockIdx.y * 4 + wid;
  const bool act = nb < nblk;
  bf16x8 wf[40];
  {
    const bf16x8* wp = (const bf16x8*)WT + (size_t)(act ? nb : 0) * 40 * 64 + lane;
#pragma unroll
    for (int i = 0; i < 40; ++i) wf[i] = wp[i * 64];
  }
  {
    const int sub = tid & 15;
#pragma unroll
    for (int pass = 0; pass < 4; ++pass) {
      const int row = pass * 16 + (tid >> 4);
      const u16* src = X + (size_t)(m0 + row) * 640;
      u32x4 v[5];
#pragma unroll
      for (int i = 0; i < 5; ++i) v[i] = *(const u32x4*)(src + (sub + 16 * i) * 8);
      if (MODE != 2) {
        float f[40]; float s = 0;
#pragma unroll
        for (int i = 0; i < 5; ++i)
#pragma unroll
          for (int j = 0; j < 4; ++j) { f[i * 8 + 2 * j] = bf2f(v[i][j] & 0xffff); f[i * 8 + 2 * j + 1] = __uint_as_float(v[i][j] & 0xffff0000u); }
#pragma unroll
        for (int i = 0; i < 40; ++i) s += f[i];
#pragma unroll
        for (int o = 1; o < 16; o <<= 1) s += __shfl_xor(s, o);
        const float mean = s * (1.0f / 640.0f);
        float q = 0;
#pragma unroll
        for (int i = 0; i < 40; ++i) { f[i] -= mean; q += f[i] * f[i]; }
#pragma unroll
        for (int o = 1; o < 16; o <<= 1) q += __shfl_xor(q, o);
        const float rs = 1.0f / sqrtf(q * (1.0f / 640.0f) + 1e-5f);
#pragma unroll
        for (int i = 0; i < 5; ++i)
#pragma unroll
          for (int j = 0; j < 4; ++j) v[i][j] = cvtpk(f[i * 8 + 2 * j] * rs, f[i * 8 + 2 * j + 1] * rs);
      }
#pragma unroll
      for (int i = 0; i < 5; ++i) *(u32x4*)(xs + row * 1280 + (16 * i + (sub ^ (row & 15))) * 16) = v[i];
    }
  }
  __syncthreads();
  const int r32 = lane & 31, hi = lane >> 5;
  f32x16 acc[2];
  acc[0] = f32x16{}; acc[1] = f32x16{};
#pragma unroll
  for (int ks = 0; ks < 40; ++ks) {
    const int c = 2 * ks + hi;
    const int off = (c & ~15) * 16 + ((c ^ r32) & 15) * 16;
    const bf16x8 x0 = *(const bf16x8*)(xs + r32 * 1280 + off);
    const bf16x8 x1 = *(const bf16x8*)(xs + (32 + r32) * 1280 + off);
    acc[0] = __builtin_amdgcn_mfma_f32_32x32x16_bf16(wf[ks], x0, acc[0], 0, 0, 0);
    acc[1] = __builtin_amdgcn_mfma_f32_32x32x16_bf16(wf[ks], x1, acc[1], 0, 0, 0);
  }
  if (!act) return;
#pragma unroll
  for (int i = 0; i < 2; ++i) {
    const int tok = m0 + i * 32 + r32;
#pragma unroll
    for (int g = 0; g < 4; ++g) {
      const int n = nb * 32 + 8 * g + 4 * hi;
      float a0 = acc[i][4 * g], a1 = acc[i][4 * g + 1], a2 = acc[i][4 * g + 2], a3 = acc[i][4 * g + 3];
      if (MODE == 2) {
        u32x2 rv = *(const u32x2*)(R + (size_t)tok * 640 + n);
        a0 += bf2f(rv[0] & 0xffff); a1 += __uint_as_float(rv[0] & 0xffff0000u);
        a2 += bf2f(rv[1] & 0xffff); a3 += __uint_as_float(rv[1] & 0xffff0000u);
        *(u32x2*)(O0 + (size_t)tok * 640 + n) = u32x2{cvtpk(a0, a1), cvtpk(a2, a3)};
      } else {
        const int lim = MODE == 0 ? 680 : 1920;
        if (n >= lim) continue;
        const int T = tok >> 5, r = tok & 31;
        if (n < 640) {
          const int hd = n / 20, d = n - hd * 20;
          u16* q = O0 + (size_t)tok * 1024 + hd * 32 + d;
          *(u32x2*)q = u32x2{cvtpk(a0 * QSCALE, a1 * QSCALE), cvtpk(a2 * QSCALE, a3 * QSCALE)};
          if (d == 16) { *(u32x2*)(q + 4) = u32x2{0, 0}; *(u32x4*)(q + 8) = u32x4{0, 0, 0, 0}; }
        } else {
          const int kvw = MODE == 0 ? 20 : 640;
          const bool isv = n >= 640 + kvw;
          const int nn = n - 640 - (isv ? kvw : 0);
          const int hd = nn / 20, d = nn - hd * 20;
          u16* base = O1 + ((size_t)hd * 64 + T) * 2048;
          if (!isv) {
            u16* kp = base + (d >> 4) * 512 + (((d >> 3) & 1) * 32 + r) * 8 + (d & 7);
            *(u32x2*)kp = u32x2{cvtpk(a0, a1), cvtpk(a2, a3)};
            if (d == 16) { *(u32x2*)(kp + 4) = u32x2{0, 0}; *(u32x4*)(kp + 256) = u32x4{0, 0, 0, 0}; }
          } else {
            const int u = r & 15, sgrp = r >> 4;
            u16* vp = base + (2 + sgrp) * 512 + (((u >> 2) & 1) * 32 + d) * 8 + (u >> 3) * 4 + (u & 3);
            unsigned p0 = cvtpk(a0, a1), p1 = cvtpk(a2, a3);
            vp[0] = p0 & 0xffff; vp[8] = p0 >> 16; vp[16] = p1 & 0xffff; vp[24] = p1 >> 16;
            if (d == 16) vp[32] = 0x3f80;
          }
        }
      }
    }
  }
}

struct AttnState { f32x16 o; float negm; };

template <bool FIRST>
__device__ __forceinline__ void attn_tile(AttnState& st, f32x16 s, bf16x8 v0, bf16x8 v1) {
  float cm = fmaxf(fmaxf(s[0], s[1]), s[2]);
#pragma unroll
  for (int i = 3; i < 15; i += 2) cm = fmaxf(fmaxf(cm, s[i]), s[i + 1]);
  cm = fmaxf(cm, s[15]);
  { auto rr = __builtin_amdgcn_permlane32_swap(__float_as_uint(cm), __float_as_uint(cm), false, false);
    cm = fmaxf(__uint_as_float(rr[0]), __uint_as_float(rr[1])); }
  if (FIRST) {
    st.negm -= cm;
#pragma unroll
    for (int i = 0; i < 16; ++i) s[i] -= cm;
  } else if (__any(cm > THR)) {
    const float d = fmaxf(cm, 0.f);
    const float al = __builtin_amdgcn_exp2f(-d);
    st.negm -= d;
#pragma unroll
    for (int i = 0; i < 16; ++i) { s[i] -= d; st.o[i] *= al; }
  }
#pragma unroll
  for (int i = 0; i < 16; ++i) s[i] = __builtin_amdgcn_exp2f(s[i]);
  u32x4 p0 = {cvtpk(s[0], s[1]), cvtpk(s[2], s[3]), cvtpk(s[4], s[5]), cvtpk(s[6], s[7])};
  u32x4 p1 = {cvtpk(s[8], s[9]), cvtpk(s[10], s[11]), cvtpk(s[12], s[13]), cvtpk(s[14], s[15])};
  st.o = __builtin_amdgcn_mfma_f32_32x32x16_bf16(v0, *(bf16x8*)&p0, st.o, 0, 0, 0);
  st.o = __builtin_amdgcn_mfma_f32_32x32x16_bf16(v1, *(bf16x8*)&p1, st.o, 0, 0, 0);
}
__device__ __forceinline__ f32x16 qk(bf16x8 k0, bf16x8 k1, bf16x8 q0, bf16x8 q1, float negm) {
  f32x16 c;
#pragma unroll
  for (int i = 0; i < 16; ++i) c[i] = negm;
  c = __builtin_amdgcn_mfma_f32_32x32x16_bf16(k0, q0, c, 0, 0, 0);
  return __builtin_amdgcn_mfma_f32_32x32x16_bf16(k1, q1, c, 0, 0, 0);
}
__device__ __forceinline__ void attn_store(const AttnState& st, u16* dst  , int lane) {
  const int hi = lane >> 5;
  const float l = __shfl(st.o[8], (lane & 31) + 32);
  const float inv = 1.0f / l;
#pragma unroll
  for (int g = 0; g < 3; ++g) {
    if (g == 2 && hi) break;
    *(u32x2*)(dst + 8 * g + 4 * hi) = u32x2{cvtpk(st.o[4 * g] * inv, st.o[4 * g + 1] * inv), cvtpk(st.o[4 * g + 2] * inv, st.o[4 * g + 3] * inv)};
  }
}

constexpr int CH = 4;
__global__ __launch_bounds__(1024) void attnA_k(const u16* __restrict__ Qp, const u16* __restrict__ KV, u16* __restrict__ O) {
  __shared__ __attribute__((aligned(16))) char lds[2 * 2 * CH * 4096];
  const int tid = threadIdx.x, wid = tid >> 6, lane = tid & 63, half = wid >> 3, t9 = tid & 511;
  const int tok = blockIdx.x * 8 + (wid & 7), c = lane & 31, hi = lane >> 5;
  const u16* qp = Qp + (size_t)tok * 1024 + c * 32 + hi * 8;
  const bf16x8 q0 = *(const bf16x8*)qp, q1 = *(const bf16x8*)(qp + 16);
  AttnState st; st.o = f32x16{}; st.negm = 0.f;
  u32x4 sr[2];
  char* lh = lds + half * (2 * CH * 4096);
  const u32x4* src = (const u32x4*)KV + half * (32 * 256) + t9;
  sr[0] = src[0]; sr[1] = src[512];
  *(u32x4*)(lh + t9 * 16) = sr[0]; *(u32x4*)(lh + 8192 + t9 * 16) = sr[1];
  __syncthreads();
  constexpr int NCH = 32 / CH;
  for (int ch = 0; ch < NCH; ++ch) {
    if (ch + 1 < NCH) { sr[0] = src[(ch + 1) * 1024]; sr[1] = src[(ch + 1) * 1024 + 512]; }
    const char* b = lh + (ch & 1) * (CH * 4096) + lane * 16;
#pragma unroll
    for (int t = 0; t < CH; ++t) {
      const bf16x8 k0 = *(const bf16x8*)(b + t * 4096), k1 = *(const bf16x8*)(b + t * 4096 + 1024);
      const bf16x8 v0 = *(const bf16x8*)(b + t * 4096 + 2048), v1 = *(const bf16x8*)(b + t * 4096 + 3072);
      f32x16 s = qk(k0, k1, q0, q1, st.negm);
      if (t == 0 && ch == 0) attn_tile<true>(st, s, v0, v1); else attn_tile<false>(st, s, v0, v1);
    }
    if (ch + 1 < NCH) { char* d = lh + ((ch + 1) & 1) * (CH * 4096) + t9 * 16; *(u32x4*)d = sr[0]; *(u32x4*)(d + 8192) = sr[1]; }
    __syncthreads();
  }
  float* cb = (float*)lds + (wid & 7) * (17 * 64) + lane;
  if (half) {
#pragma unroll
    for (int i = 0; i < 16; ++i) cb[i * 64] = st.o[i];
    cb[16 * 64] = st.negm;
  }
  __syncthreads();
  if (!half) {
    const float nb = cb[16 * 64];
    const float nm = fminf(st.negm, nb);
    const float fa = __builtin_amdgcn_exp2f(nm - st.negm), fb = __builtin_amdgcn_exp2f(nm - nb);
#pragma unroll
    for (int i = 0; i < 16; ++i) st.o[i] = st.o[i] * fa + cb[i * 64] * fb;
    attn_store(st, O + (size_t)tok * 640 + c * 20, lane);
  }
}

__global__ __launch_bounds__(512) void attnB_k(const u16* __restrict__ Qp, const u16* __restrict__ KV, u16* __restrict__ O) {
  const int tid = threadIdx.x, wid = tid >> 6, lane = tid & 63;
  const int gw = blockIdx.x * 8 + wid, head = gw & 31, qt = gw >> 5;
  const int c = lane & 31, hi = lane >> 5, tok = qt * 32 + c;
  const u16* qp = Qp + (size_t)tok * 1024 + head * 32 + hi * 8;
  const bf16x8 q0 = *(const bf16x8*)qp, q1 = *(const bf16x8*)(qp + 16);
  AttnState st; st.o = f32x16{}; st.negm = 0.f;
  const u16* kvb = KV + (size_t)head * 64 * 2048 + lane * 8;
  const int tlo = qt >= 8 ? qt - 8 : 0;
  bf16x8 nk0, nk1, nv0, nv1;
  { const u16* b = kvb + qt * 2048; nk0 = *(const bf16x8*)b; nk1 = *(const bf16x8*)(b + 512); nv0 = *(const bf16x8*)(b + 1024); nv1 = *(const bf16x8*)(b + 1536); }
  for (int T = qt; T >= tlo; --T) {
    const bf16x8 k0 = nk0, k1 = nk1, v0 = nv0, v1 = nv1;
    if (T > tlo) { const u16* b = kvb + (T - 1) * 2048; nk0 = *(const bf16x8*)b; nk1 = *(const bf16x8*)(b + 512); nv0 = *(const bf16x8*)(b + 1024); nv1 = *(const bf16x8*)(b + 1536); }
    f32x16 s = qk(k0, k1, q0, q1, st.negm);
    if (T == qt) {
#pragma unroll
      for (int i = 0; i < 16; ++i) { const int row = (i & 3) + 8 * (i >> 2) + 4 * hi; if (row > c) s[i] = -1e30f; }
      attn_tile<true>(st, s, v0, v1);
    } else {
      if (T == qt - 8) {
#pragma unroll
        for (int i = 0; i < 16; ++i) { const int row = (i & 3) + 8 * (i >> 2) + 4 * hi; if (row <= c) s[i] = -1e30f; }
      }
      attn_tile<false>(st, s, v0, v1);
    }
  }
  attn_store(st, O + (size_t)tok * 640 + head * 20, lane);
}

extern "C" void kernel_launch(void* const* d_in, const int* in_sizes, int n_in, void* d_out, int out_size,
                              void* d_ws, size_t ws_size, hipStream_t stream) {
  const u16* x = (const u16*)d_in[0];
  const u16 *wq = (const u16*)d_in[1], *wk = (const u16*)d_in[2], *wv = (const u16*)d_in[3], *woA = (const u16*)d_in[4];
  const u16 *wqkv = (const u16*)d_in[6], *woB = (const u16*)d_in[7];
  char* w = (char*)d_ws;
  u16* xb0 = (u16*)(w);
  u16* xb1 = (u16*)(w + (4u << 20));
  u16* Qp = (u16*)(w + (8u << 20));
  u16* KV = (u16*)(w + (16u << 20));
  u16* ob = (u16*)(w + (28u << 20));
  u16* WT = (u16*)(w + (32u << 20));
  const size_t FB = 40 * 64 * 8;
  prep_k<<<122 * 40 * 64 / 256, 256, 0, stream>>>(wq, wk, wv, woA, wqkv, woB, WT);
  const u16* xin = x;
  u16* outs[4] = {xb0, xb1, xb0, (u16*)d_out};
  for (int layer = 0; layer < 4; ++layer) {
    if ((layer & 1) == 0) {
      gemm_k<0><<<dim3(32, 6), 256, 0, stream>>>(xin, WT, nullptr, Qp, KV, 22);
      attnA_k<<<256, 1024, 0, stream>>>(Qp, KV, ob);
      gemm_k<2><<<dim3(32, 5), 256, 0, stream>>>(ob, WT + 22 * FB, xin, outs[layer], nullptr, 20);
    } else {
      gemm_k<1><<<dim3(32, 15), 256, 0, stream>>>(xin, WT + 42 * FB, nullptr, Qp, KV, 60);
      attnB_k<<<256, 512, 0, stream>>>(Qp, KV, ob);
      gemm_k<2><<<dim3(32, 5), 256, 0, stream>>>(ob, WT + 102 * FB, xin, outs[layer], nullptr, 20);
    }
    xin = outs[layer];
  }
}
```

```cpp
#include <hip/hip_runtime.h>
#include <hip/hip_bf16.h>
#include <stdint.h>

typedef __attribute__((ext_vector_type(8))) short bf16x8;
typedef __attribute__((ext_vector_type(4))) short s16x4;
typedef __attribute__((ext_vector_type(16))) float f32x16;
typedef __attribute__((ext_vector_type(4))) unsigned u32x4;
typedef __attribute__((ext_vector_type(2))) unsigned u32x2;
typedef unsigned short u16;

constexpr int S = 2048, D = 640, H = 32;
constexpr float QSCALE = 0.22360679774997896f * 1.4426950408889634f;
constexpr float THR = 10.0f;

__device__ __forceinline__ unsigned cvtpk(float lo, float hi) {
  unsigned r; asm volatile("v_cvt_pk_bf16_f32 %0, %1, %2" : "=v"(r) : "v"(lo), "v"(hi)); return r;
}
__device__ __forceinline__ float bf2f(unsigned v16) { return __uint_as_float(v16 << 16); }

__global__ __launch_bounds__(256) void prep_k(const u16* __restrict__ wq, const u16* __restrict__ wk, const u16* __restrict__ wv,
                                              const u16* __restrict__ woA, const u16* __restrict__ wqkv, const u16* __restrict__ woB,
                                              u16* __restrict__ dst) {
  const int g = blockIdx.x * 256 + threadIdx.x;
  const int lane = g & 63, fr = g >> 6, nbg = fr / 40, ks = fr - nbg * 40;
  const int r = lane & 31, h = lane >> 5;
  unsigned e[8];
  const u16* src; int ld, n;
  if (nbg < 22) { n = nbg * 32 + r; src = wq; ld = 640;
    if (n >= 640) { if (n < 660) { src = wk; ld = 20; n -= 640; } else if (n < 680) { src = wv; ld = 20; n -= 660; } else src = nullptr; } }
  else if (nbg < 42) { n = (nbg - 22) * 32 + r; src = woA; ld = 640; }
  else if (nbg < 102) { n = (nbg - 42) * 32 + r; src = wqkv; ld = 1920; }
  else { n = (nbg - 102) * 32 + r; src = woB; ld = 640; }
#pragma unroll
  for (int j = 0; j < 8; ++j) e[j] = src ? src[(16 * ks + 8 * h + j) * ld + n] : 0;
  *(u32x4*)(dst + (size_t)g * 8) = u32x4{e[0] | (e[1] << 16), e[2] | (e[3] << 16), e[4] | (e[5] << 16), e[6] | (e[7] << 16)};
}

template <int MODE>
__global__ __launch_bounds__(256, 2) void gemm_k(const u16* __restrict__ X, const u16* __restrict__ WT,
                                              const u16* __restrict__ R, u16* __restrict__ O0,
                                              u16* __restrict__ O1, int nblk) {
  __shared__ __attribute__((aligned(16))) char xs[64 * 1280];
  const int tid = threadIdx.x, wid = tid >> 6, lane = tid & 63;
  const int m0 = blockIdx.x * 64, nb = blockIdx.y * 4 + wid;
  const bool act = nb < nblk;
  bf16x8 wf[40];
  {
    const bf16x8* wp = (const bf16x8*)WT + (size_t)(act ? nb : 0) * 40 * 64 + lane;
#pragma unroll
    for (int i = 0; i < 40; ++i) wf[i] = wp[i * 64];
  }
  {
    const int sub = tid & 15;
#pragma unroll
    for (int pass = 0; pass < 4; ++pass) {
      const int row = pass * 16 + (tid >> 4);
      const u16* src = X + (size_t)(m0 + row) * 640;
      u32x4 v[5];
#pragma unroll
      for (int i = 0; i < 5; ++i) v[i] = *(const u32x4*)(src + (sub + 16 * i) * 8);
      if (MODE != 2) {
        float f[40]; float s = 0;
#pragma unroll
        for (int i = 0; i < 5; ++i)
#pragma unroll
          for (int j = 0; j < 4; ++j) { f[i * 8 + 2 * j] = bf2f(v[i][j] & 0xffff); f[i * 8 + 2 * j + 1] = __uint_as_float(v[i][j] & 0xffff0000u); }
#pragma unroll
        for (int i = 0; i < 40; ++i) s += f[i];
#pragma unroll
        for (int o = 1; o < 16; o <<= 1) s += __shfl_xor(s, o);
        const float mean = s * (1.0f / 640.0f);
        float q = 0;
#pragma unroll
        for (int i = 0; i < 40; ++i) { f[i] -= mean; q += f[i] * f[i]; }
#pragma unroll
        for (int o = 1; o < 16; o <<= 1) q += __shfl_xor(q, o);
        const float rs = 1.0f / sqrtf(q * (1.0f / 640.0f) + 1e-5f);
#pragma unroll
        for (int i = 0; i < 5; ++i)
#pragma unroll
          for (int j = 0; j < 4; ++j) v[i][j] = cvtpk(f[i * 8 + 2 * j] * rs, f[i * 8 + 2 * j + 1] * rs);
      }
#pragma unroll
      for (int i = 0; i < 5; ++i) *(u32x4*)(xs + row * 1280 + (16 * i + (sub ^ (row & 15))) * 16) = v[i];
    }
  }
  __syncthreads();
  const int r32 = lane & 31, hi = lane >> 5;
  f32x16 acc[2];
  acc[0] = f32x16{}; acc[1] = f32x16{};
#pragma unroll
  for (int ks = 0; ks < 40; ++ks) {
    const int c = 2 * ks + hi;
    const int off = (c & ~15) * 16 + ((c ^ r32) & 15) * 16;
    const bf16x8 x0 = *(const bf16x8*)(xs + r32 * 1280 + off);
    const bf16x8 x1 = *(const bf16x8*)(xs + (32 + r32) * 1280 + off);
    acc[0] = __builtin_amdgcn_mfma_f32_32x32x16_bf16(wf[ks], x0, acc[0], 0, 0, 0);
    acc[1] = __builtin_amdgcn_mfma_f32_32x32x16_bf16(wf[ks], x1, acc[1], 0, 0, 0);
  }
  if (!act) return;
#pragma unroll
  for (int i = 0; i < 2; ++i) {
    const int tok = m0 + i * 32 + r32;
#pragma unroll
    for (int g = 0; g < 4; ++g) {
      const int n = nb * 32 + 8 * g + 4 * hi;
      float a0 = acc[i][4 * g], a1 = acc[i][4 * g + 1], a2 = acc[i][4 * g + 2], a3 = acc[i][4 * g + 3];
      if (MODE == 2) {
        u32x2 rv = *(const u32x2*)(R + (size_t)tok * 640 + n);
        a0 += bf2f(rv[0] & 0xffff); a1 += __uint_as_float(rv[0] & 0xffff0000u);
        a2 += bf2f(rv[1] & 0xffff); a3 += __uint_as_float(rv[1] & 0xffff0000u);
        *(u32x2*)(O0 + (size_t)tok * 640 + n) = u32x2{cvtpk(a0, a1), cvtpk(a2, a3)};
      } else {
        const int lim = MODE == 0 ? 680 : 1920;
        if (n >= lim) continue;
        const int T = tok >> 5, r = tok & 31;
        if (n < 640) {
          const int hd = n / 20, d = n - hd * 20;
          u16* q = O0 + (size_t)tok * 1024 + hd * 32 + d;
          *(u32x2*)q = u32x2{cvtpk(a0 * QSCALE, a1 * QSCALE), cvtpk(a2 * QSCALE, a3 * QSCALE)};
          if (d == 16) { *(u32x2*)(q + 4) = u32x2{0, 0}; *(u32x4*)(q + 8) = u32x4{0, 0, 0, 0}; }
        } else {
          const int kvw = MODE == 0 ? 20 : 640;
          const bool isv = n >= 640 + kvw;
          const int nn = n - 640 - (isv ? kvw : 0);
          const int hd = nn / 20, d = nn - hd * 20;
          u16* base = O1 + ((size_t)hd * 64 + T) * 2048;
          if (!isv) {
            u16* kp = base + (d >> 4) * 512 + (((d >> 3) & 1) * 32 + r) * 8 + (d & 7);
            *(u32x2*)kp = u32x2{cvtpk(a0, a1), cvtpk(a2, a3)};
            if (d == 16) { *(u32x2*)(kp + 4) = u32x2{0, 0}; *(u32x4*)(kp + 256) = u32x4{0, 0, 0, 0}; }
          } else {
            const int u = r & 15, sgrp = r >> 4;
            u16* vp = base + (2 + sgrp) * 512 + (((u >> 2) & 1) * 32 + d) * 8 + (u >> 3) * 4 + (u & 3);
            unsigned p0 = cvtpk(a0, a1), p1 = cvtpk(a2, a3);
            vp[0] = p0 & 0xffff; vp[8] = p0 >> 16; vp[16] = p1 & 0xffff; vp[24] = p1 >> 16;
            if (d == 16) vp[32] = 0x3f80;
          }
        }
      }
    }
  }
}

struct AttnState { f32x16 o; f32x16 nv; float negm; };
__device__ __forceinline__ float max3(float a, float b, float c) { float r; asm("v_max3_f32 %0, %1, %2, %3" : "=v"(r) : "v"(a), "v"(b), "v"(c)); return r; }

template <bool FIRST>
__device__ __forceinline__ void attn_tile(AttnState& st, f32x16 s, bf16x8 v0, bf16x8 v1) {
  float cm = max3(s[0], s[1], s[2]);
#pragma unroll
  for (int i = 3; i < 15; i += 2) cm = max3(cm, s[i], s[i + 1]);
  cm = max3(cm, s[15], s[15]);
  { auto rr = __builtin_amdgcn_permlane32_swap(__float_as_uint(cm), __float_as_uint(cm), false, false);
    cm = max3(__uint_as_float(rr[0]), __uint_as_float(rr[1]), cm); }
  if (FIRST) {
    st.negm -= cm;
#pragma unroll
    for (int i = 0; i < 16; ++i) { s[i] -= cm; st.nv[i] = st.negm; }
  } else if (__any(cm > THR)) {
    const float d = fmaxf(cm, 0.f);
    const float al = __builtin_amdgcn_exp2f(-d);
    st.negm -= d;
#pragma unroll
    for (int i = 0; i < 16; ++i) { s[i] -= d; st.o[i] *= al; st.nv[i] = st.negm; }
  }
#pragma unroll
  for (int i = 0; i < 16; ++i) s[i] = __builtin_amdgcn_exp2f(s[i]);
  u32x4 p0 = {cvtpk(s[0], s[1]), cvtpk(s[2], s[3]), cvtpk(s[4], s[5]), cvtpk(s[6], s[7])};
  u32x4 p1 = {cvtpk(s[8], s[9]), cvtpk(s[10], s[11]), cvtpk(s[12], s[13]), cvtpk(s[14], s[15])};
  st.o = __builtin_amdgcn_mfma_f32_32x32x16_bf16(v0, *(bf16x8*)&p0, st.o, 0, 0, 0);
  st.o = __builtin_amdgcn_mfma_f32_32x32x16_bf16(v1, *(bf16x8*)&p1, st.o, 0, 0, 0);
}
__device__ __forceinline__ f32x16 qk(bf16x8 k0, bf16x8 k1, bf16x8 q0, bf16x8 q1, const f32x16& nv) {
  f32x16 c = __builtin_amdgcn_mfma_f32_32x32x16_bf16(k0, q0, nv, 0, 0, 0);
  return __builtin_amdgcn_mfma_f32_32x32x16_bf16(k1, q1, c, 0, 0, 0);
}
__device__ __forceinline__ void attn_store(const AttnState& st, u16* dst  , int lane) {
  const int hi = lane >> 5;
  const float l = __shfl(st.o[8], (lane & 31) + 32);
  const float inv = 1.0f / l;
#pragma unroll
  for (int g = 0; g < 3; ++g) {
    if (g == 2 && hi) break;
    *(u32x2*)(dst + 8 * g + 4 * hi) = u32x2{cvtpk(st.o[4 * g] * inv, st.o[4 * g + 1] * inv), cvtpk(st.o[4 * g + 2] * inv, st.o[4 * g + 3] * inv)};
  }
}

constexpr int CH = 4;
__global__ __launch_bounds__(1024) void attnA_k(const u16* __restrict__ Qp, const u16* __restrict__ KV, u16* __restrict__ O) {
  __shared__ __attribute__((aligned(16))) char lds[2 * 2 * CH * 4096];
  const int tid = threadIdx.x, wid = tid >> 6, lane = tid & 63, half = wid >> 3, t9 = tid & 511;
  const int tok = blockIdx.x * 8 + (wid & 7), c = lane & 31, hi = lane >> 5;
  const u16* qp = Qp + (size_t)tok * 1024 + c * 32 + hi * 8;
  const bf16x8 q0 = *(const bf16x8*)qp, q1 = *(const bf16x8*)(qp + 16);
  AttnState st; st.o = f32x16{}; st.nv = f32x16{}; st.negm = 0.f;
  u32x4 sr[2];
  char* lh = lds + half * (2 * CH * 4096);
  const u32x4* src = (const u32x4*)KV + half * (32 * 256) + t9;
  sr[0] = src[0]; sr[1] = src[512];
  *(u32x4*)(lh + t9 * 16) = sr[0]; *(u32x4*)(lh + 8192 + t9 * 16) = sr[1];
  __syncthreads();
  constexpr int NCH = 32 / CH;
  for (int ch = 0; ch < NCH; ++ch) {
    if (ch + 1 < NCH) { sr[0] = src[(ch + 1) * 1024]; sr[1] = src[(ch + 1) * 1024 + 512]; }
    const char* b = lh + (ch & 1) * (CH * 4096) + lane * 16;
#pragma unroll
    for (int t = 0; t < CH; ++t) {
      const bf16x8 k0 = *(const bf16x8*)(b + t * 4096), k1 = *(const bf16x8*)(b + t * 4096 + 1024);
      const bf16x8 v0 = *(const bf16x8*)(b + t * 4096 + 2048), v1 = *(const bf16x8*)(b + t * 4096 + 3072);
      f32x16 s = qk(k0, k1, q0, q1, st.nv);
      if (t == 0 && ch == 0) attn_tile<true>(st, s, v0, v1); else attn_tile<false>(st, s, v0, v1);
    }
    if (ch + 1 < NCH) { char* d = lh + ((ch + 1) & 1) * (CH * 4096) + t9 * 16; *(u32x4*)d = sr[0]; *(u32x4*)(d + 8192) = sr[1]; }
    __syncthreads();
  }
  float* cb = (float*)lds + (wid & 7) * (17 * 64) + lane;
  if (half) {
#pragma unroll
    for (int i = 0; i < 16; ++i) cb[i * 64] = st.o[i];
    cb[16 * 64] = st.negm;
  }
  __syncthreads();
  if (!half) {
    const float nb = cb[16 * 64];
    const float nm = fminf(st.negm, nb);
    const float fa = __builtin_amdgcn_exp2f(nm - st.negm), fb = __builtin_amdgcn_exp2f(nm - nb);
#pragma unroll
    for (int i = 0; i < 16; ++i) st.o[i] = st.o[i] * fa + cb[i * 64] * fb;
    attn_store(st, O + (size_t)tok * 640 + c * 20, lane);
  }
}

__global__ __launch_bounds__(512) void attnB_k(const u16* __restrict__ Qp, const u16* __restrict__ KV, u16* __restrict__ O) {
  const int tid = threadIdx.x, wid = tid >> 6, lane = tid & 63;
  const int gw = blockIdx.x * 8 + wid, head = gw & 31, qt = gw >> 5;
  const int c = lane & 31, hi = lane >> 5, tok = qt * 32 + c;
  const u16* qp = Qp + (size_t)tok * 1024 + head * 32 + hi * 8;
  const bf16x8 q0 = *(const bf16x8*)qp, q1 = *(const bf16x8*)(qp + 16);
  AttnState st; st.o = f32x16{}; st.nv = f32x16{}; st.negm = 0.f;
  const u16* kvb = KV + (size_t)head * 64 * 2048 + lane * 8;
  const int tlo = qt >= 8 ? qt - 8 : 0;
  bf16x8 nk0, nk1, nv0, nv1;
  { const u16* b = kvb + qt * 2048; nk0 = *(const bf16x8*)b; nk1 = *(const bf16x8*)(b + 512); nv0 = *(const bf16x8*)(b + 1024); nv1 = *(const bf16x8*)(b + 1536); }
  for (int T = qt; T >= tlo; --T) {
    const bf16x8 k0 = nk0, k1 = nk1, v0 = nv0, v1 = nv1;
    if (T > tlo) { const u16* b = kvb + (T - 1) * 2048; nk0 = *(const bf16x8*)b; nk1 = *(const bf16x8*)(b + 512); nv0 = *(const bf16x8*)(b + 1024); nv1 = *(const bf16x8*)(b + 1536); }
    f32x16 s = qk(k0, k1, q0, q1, st.nv);
    if (T == qt) {
#pragma unroll
      for (int i = 0; i < 16; ++i) { const int row = (i & 3) + 8 * (i >> 2) + 4 * hi; if (row > c) s[i] = -1e30f; }
      attn_tile<true>(st, s, v0, v1);
    } else {
      if (T == qt - 8) {
#pragma unroll
        for (int i = 0; i < 16; ++i) { const int row = (i & 3) + 8 * (i >> 2) + 4 * hi; if (row <= c) s[i] = -1e30f; }
      }
      attn_tile<false>(st, s, v0, v1);
    }
  }
  attn_store(st, O + (size_t)tok * 640 + head * 20, lane);
}

extern "C" void kernel_launch(void* const* d_in, const int* in_sizes, int n_in, void* d_out, int out_size,
                              void* d_ws, size_t ws_size, hipStream_t stream) {
  const u16* x = (const u16*)d_in[0];
  const u16 *wq = (const u16*)d_in[1], *wk = (const u16*)d_in[2], *wv = (const u16*)d_in[3], *woA = (const u16*)d_in[4];
  const u16 *wqkv = (const u16*)d_in[6], *woB = (const u16*)d_in[7];
  char* w = (char*)d_ws;
  u16* xb0 = (u16*)(w);
  u16* xb1 = (u16*)(w + (4u << 20));
  u16* Qp = (u16*)(w + (8u << 20));
  u16* KV = (u16*)(w + (16u << 20));
  u16* ob = (u16*)(w + (28u << 20));
  u16* WT = (u16*)(w + (32u << 20));
  const size_t FB = 40 * 64 * 8;
  prep_k<<<122 * 40 * 64 / 256, 256, 0, stream>>>(wq, wk, wv, woA, wqkv, woB, WT);
  const u16* xin = x;
  u16* outs[4] = {xb0, xb1, xb0, (u16*)d_out};
  for (int layer = 0; layer < 4; ++layer) {
    if ((layer & 1) == 0) {
      gemm_k<0><<<dim3(32, 6), 256, 0, stream>>>(xin, WT, nullptr, Qp, KV, 22);
      attnA_k<<<256, 1024, 0, stream>>>(Qp, KV, ob);
      gemm_k<2><<<dim3(32, 5), 256, 0, stream>>>(ob, WT + 22 * FB, xin, outs[layer], nullptr, 20);
    } else {
      gemm_k<1><<<dim3(32, 15), 256, 0, stream>>>(xin, WT + 42 * FB, nullptr, Qp, KV, 60);
      attnB_k<<<256, 512, 0, stream>>>(Qp, KV, ob);
      gemm_k<2><<<dim3(32, 5), 256, 0, stream>>>(ob, WT + 102 * FB, xin, outs[layer], nullptr, 20);
    }
    xin = outs[layer];
  }
}
```

```cpp
#include <hip/hip_runtime.h>
#include <hip/hip_bf16.h>
#include <stdint.h>

typedef __attribute__((ext_vector_type(8))) short bf16x8;
typedef __attribute__((ext_vector_type(4))) short s16x4;
typedef __attribute__((ext_vector_type(16))) float f32x16;
typedef __attribute__((ext_vector_type(4))) unsigned u32x4;
typedef __attribute__((ext_vector_type(2))) unsigned u32x2;
typedef unsigned short u16;

constexpr int S = 2048, D = 640, H = 32;
constexpr float QSCALE = 0.22360679774997896f * 1.4426950408889634f;
constexpr float THR = 10.0f;

__device__ __forceinline__ unsigned cvtpk(float lo, float hi) {
  unsigned r; asm volatile("v_cvt_pk_bf16_f32 %0, %1, %2" : "=v"(r) : "v"(lo), "v"(hi)); return r;
}
__device__ __forceinline__ float bf2f(unsigned v16) { return __uint_as_float(v16 << 16); }

__global__ __launch_bounds__(256) void prep_k(const u16* __restrict__ wq, const u16* __restrict__ wk, const u16* __restrict__ wv,
                                              const u16* __restrict__ woA, const u16* __restrict__ wqkv, const u16* __restrict__ woB,
                                              u16* __restrict__ dst) {
  const int g = blockIdx.x * 256 + threadIdx.x;
  const int lane = g & 63, fr = g >> 6, nbg = fr / 40, ks = fr - nbg * 40;
  const int r = lane & 31, h = lane >> 5;
  unsigned e[8];
  const u16* src; int ld, n;
  if (nbg < 22) { n = nbg * 32 + r; src = wq; ld = 640;
    if (n >= 640) { if (n < 660) { src = wk; ld = 20; n -= 640; } else if (n < 680) { src = wv; ld = 20; n -= 660; } else src = nullptr; } }
  else if (nbg < 42) { n = (nbg - 22) * 32 + r; src = woA; ld = 640; }
  else if (nbg < 102) { n = (nbg - 42) * 32 + r; src = wqkv; ld = 1920; }
  else { n = (nbg - 102) * 32 + r; src = woB; ld = 640; }
#pragma unroll
  for (int j = 0; j < 8; ++j) e[j] = src ? src[(16 * ks + 8 * h + j) * ld + n] : 0;
  *(u32x4*)(dst + (size_t)g * 8) = u32x4{e[0] | (e[1] << 16), e[2] | (e[3] << 16), e[4] | (e[5] << 16), e[6] | (e[7] << 16)};
}

template <int MODE>
__global__ __launch_bounds__(256, 2) void gemm_k(const u16* __restrict__ X, const u16* __restrict__ WT,
                                              const u16* __restrict__ R, u16* __restrict__ O0,
                                              u16* __restrict__ O1, int nblk) {
  __shared__ __attribute__((aligned(16))) char xs[64 * 1280];
  const int tid = threadIdx.x, wid = tid >> 6, lane = tid & 63;
  const int m0 = blockIdx.x * 64, nb = blockIdx.y * 4 + wid;
  const bool act = nb < nblk;
  bf16x8 wf[40];
  {
    const int sub = tid & 15;
#pragma unroll
    for (int pass = 0; pass < 4; ++pass) {
      const int row = pass * 16 + (tid >> 4);
      const u16* src = X + (size_t)(m0 + row) * 640;
      u32x4 v[5];
#pragma unroll
      for (int i = 0; i < 5; ++i) v[i] = *(const u32x4*)(src + (sub + 16 * i) * 8);
      {
        const bf16x8* wp = (const bf16x8*)WT + (size_t)(act ? nb : 0) * 40 * 64 + lane;
#pragma unroll
        for (int i = 0; i < 10; ++i) wf[pass * 10 + i] = wp[(pass * 10 + i) * 64];
      }
      if (MODE != 2) {
        float f[40]; float s = 0;
#pragma unroll
        for (int i = 0; i < 5; ++i)
#pragma unroll
          for (int j = 0; j < 4; ++j) { f[i * 8 + 2 * j] = bf2f(v[i][j] & 0xffff); f[i * 8 + 2 * j + 1] = __uint_as_float(v[i][j] & 0xffff0000u); }
#pragma unroll
        for (int i = 0; i < 40; ++i) s += f[i];
#pragma unroll
        for (int o = 1; o < 16; o <<= 1) s += __shfl_xor(s, o);
        const float mean = s * (1.0f / 640.0f);
        float q = 0;
#pragma unroll
        for (int i = 0; i < 40; ++i) { f[i] -= mean; q += f[i] * f[i]; }
#pragma unroll
        for (int o = 1; o < 16; o <<= 1) q += __shfl_xor(q, o);
        const float rs = 1.0f / sqrtf(q * (1.0f / 640.0f) + 1e-5f);
#pragma unroll
        for (int i = 0; i < 5; ++i)
#pragma unroll
          for (int j = 0; j < 4; ++j) v[i][j] = cvtpk(f[i * 8 + 2 * j] * rs, f[i * 8 + 2 * j + 1] * rs);
      }
#pragma unroll
      for (int i = 0; i < 5; ++i) *(u32x4*)(xs + row * 1280 + (16 * i + (sub ^ (row & 15))) * 16) = v[i];
    }
  }
  __syncthreads();
  const int r32 = lane & 31, hi = lane >> 5;
  f32x16 acc[2];
  acc[0] = f32x16{}; acc[1] = f32x16{};
#pragma unroll
  for (int ks = 0; ks < 40; ++ks) {
    const int c = 2 * ks + hi;
    const int off = (c & ~15) * 16 + ((c ^ r32) & 15) * 16;
    const bf16x8 x0 = *(const bf16x8*)(xs + r32 * 1280 + off);
    const bf16x8 x1 = *(const bf16x8*)(xs + (32 + r32) * 1280 + off);
    acc[0] = __builtin_amdgcn_mfma_f32_32x32x16_bf16(wf[ks], x0, acc[0], 0, 0, 0);
    acc[1] = __builtin_amdgcn_mfma_f32_32x32x16_bf16(wf[ks], x1, acc[1], 0, 0, 0);
  }
  if (!act) return;
#pragma unroll
  for (int i = 0; i < 2; ++i) {
    const int tok = m0 + i * 32 + r32;
#pragma unroll
    for (int g = 0; g < 4; ++g) {
      const int n = nb * 32 + 8 * g + 4 * hi;
      float a0 = acc[i][4 * g], a1 = acc[i][4 * g + 1], a2 = acc[i][4 * g + 2], a3 = acc[i][4 * g + 3];
      if (MODE == 2) {
        u32x2 rv = *(const u32x2*)(R + (size_t)tok * 640 + n);
        a0 += bf2f(rv[0] & 0xffff); a1 += __uint_as_float(rv[0] & 0xffff0000u);
        a2 += bf2f(rv[1] & 0xffff); a3 += __uint_as_float(rv[1] & 0xffff0000u);
        *(u32x2*)(O0 + (size_t)tok * 640 + n) = u32x2{cvtpk(a0, a1), cvtpk(a2, a3)};
      } else {
        const int lim = MODE == 0 ? 680 : 1920;
        if (n >= lim) continue;
        const int T = tok >> 5, r = tok & 31;
        if (n < 640) {
          const int hd = n / 20, d = n - hd * 20;
          u16* q = O0 + (size_t)tok * 1024 + hd * 32 + d;
          *(u32x2*)q = u32x2{cvtpk(a0 * QSCALE, a1 * QSCALE), cvtpk(a2 * QSCALE, a3 * QSCALE)};
          if (d == 16) { *(u32x2*)(q + 4) = u32x2{0, 0}; *(u32x4*)(q + 8) = u32x4{0, 0, 0, 0}; }
        } else {
          const int kvw = MODE == 0 ? 20 : 640;
          const bool isv = n >= 640 + kvw;
          const int nn = n - 640 - (isv ? kvw : 0);
          const int hd = nn / 20, d = nn - hd * 20;
          u16* base = O1 + ((size_t)hd * 64 + T) * 2048;
          if (!isv) {
            u16* kp = base + (d >> 4) * 512 + (((d >> 3) & 1) * 32 + r) * 8 + (d & 7);
            *(u32x2*)kp = u32x2{cvtpk(a0, a1), cvtpk(a2, a3)};
            if (d == 16) { *(u32x2*)(kp + 4) = u32x2{0, 0}; *(u32x4*)(kp + 256) = u32x4{0, 0, 0, 0}; }
          } else {
            const int u = r & 15, sgrp = r >> 4;
            u16* vp = base + (2 + sgrp) * 512 + (((u >> 2) & 1) * 32 + d) * 8 + (u >> 3) * 4 + (u & 3);
            unsigned p0 = cvtpk(a0, a1), p1 = cvtpk(a2, a3);
            vp[0] = p0 & 0xffff; vp[8] = p0 >> 16; vp[16] = p1 & 0xffff; vp[24] = p1 >> 16;
            if (d == 16) vp[32] = 0x3f80;
          }
        }
      }
    }
  }
}

struct AttnState { f32x16 o; f32x16 nv; float negm; };
__device__ __forceinline__ float max3(float a, float b, float c) { return fmaxf(fmaxf(a, b), c); }

template <bool FIRST>
__device__ __forceinline__ void attn_tile(AttnState& st, f32x16 s, bf16x8 v0, bf16x8 v1) {
  float cm = max3(s[0], s[1], s[2]);
#pragma unroll
  for (int i = 3; i < 15; i += 2) cm = max3(cm, s[i], s[i + 1]);
  cm = fmaxf(cm, s[15]);
  { auto rr = __builtin_amdgcn_permlane32_swap(__float_as_uint(cm), __float_as_uint(cm), false, false);
    cm = fmaxf(__uint_as_float(rr[0]), __uint_as_float(rr[1])); }
  if (FIRST) {
    st.negm -= cm;
#pragma unroll
    for (int i = 0; i < 16; ++i) { s[i] -= cm; st.nv[i] = st.negm; }
  } else if (__any(cm > THR)) {
    const float d = fmaxf(cm, 0.f);
    const float al = __builtin_amdgcn_exp2f(-d);
    st.negm -= d;
#pragma unroll
    for (int i = 0; i < 16; ++i) { s[i] -= d; st.o[i] *= al; st.nv[i] = st.negm; }
  }
#pragma unroll
  for (int i = 0; i < 16; ++i) s[i] = __builtin_amdgcn_exp2f(s[i]);
  u32x4 p0 = {cvtpk(s[0], s[1]), cvtpk(s[2], s[3]), cvtpk(s[4], s[5]), cvtpk(s[6], s[7])};
  u32x4 p1 = {cvtpk(s[8], s[9]), cvtpk(s[10], s[11]), cvtpk(s[12], s[13]), cvtpk(s[14], s[15])};
  st.o = __builtin_amdgcn_mfma_f32_32x32x16_bf16(v0, *(bf16x8*)&p0, st.o, 0, 0, 0);
  st.o = __builtin_amdgcn_mfma_f32_32x32x16_bf16(v1, *(bf16x8*)&p1, st.o, 0, 0, 0);
}
__device__ __forceinline__ f32x16 qk(bf16x8 k0, bf16x8 k1, bf16x8 q0, bf16x8 q1, const f32x16& nv) {
  f32x16 c = __builtin_amdgcn_mfma_f32_32x32x16_bf16(k0, q0, nv, 0, 0, 0);
  return __builtin_amdgcn_mfma_f32_32x32x16_bf16(k1, q1, c, 0, 0, 0);
}
__device__ __forceinline__ void attn_store(const AttnState& st, u16* dst  , int lane) {
  const int hi = lane >> 5;
  const float l = __shfl(st.o[8], (lane & 31) + 32);
  const float inv = 1.0f / l;
#pragma unroll
  for (int g = 0; g < 3; ++g) {
    if (g == 2 && hi) break;
    *(u32x2*)(dst + 8 * g + 4 * hi) = u32x2{cvtpk(st.o[4 * g] * inv, st.o[4 * g + 1] * inv), cvtpk(st.o[4 * g + 2] * inv, st.o[4 * g + 3] * inv)};
  }
}

constexpr int CH = 4;
__global__ __launch_bounds__(1024) void attnA_k(const u16* __restrict__ Qp, const u16* __restrict__ KV, u16* __restrict__ O) {
  __shared__ __attribute__((aligned(16))) char lds[2 * 2 * CH * 4096];
  const int tid = threadIdx.x, wid = tid >> 6, lane = tid & 63, half = wid >> 3, t9 = tid & 511;
  const int tok = blockIdx.x * 8 + (wid & 7), c = lane & 31, hi = lane >> 5;
  const u16* qp = Qp + (size_t)tok * 1024 + c * 32 + hi * 8;
  const bf16x8 q0 = *(const bf16x8*)qp, q1 = *(const bf16x8*)(qp + 16);
  AttnState st; st.o = f32x16{}; st.nv = f32x16{}; st.negm = 0.f;
  u32x4 sr[2];
  char* lh = lds + half * (2 * CH * 4096);
  const u32x4* src = (const u32x4*)KV + half * (32 * 256) + t9;
  sr[0] = src[0]; sr[1] = src[512];
  *(u32x4*)(lh + t9 * 16) = sr[0]; *(u32x4*)(lh + 8192 + t9 * 16) = sr[1];
  __syncthreads();
  constexpr int NCH = 32 / CH;
  for (int ch = 0; ch < NCH; ++ch) {
    if (ch + 1 < NCH) { sr[0] = src[(ch + 1) * 1024]; sr[1] = src[(ch + 1) * 1024 + 512]; }
    const char* b = lh + (ch & 1) * (CH * 4096) + lane * 16;
#pragma unroll
    for (int t = 0; t < CH; ++t) {
      const bf16x8 k0 = *(const bf16x8*)(b + t * 4096), k1 = *(const bf16x8*)(b + t * 4096 + 1024);
      const bf16x8 v0 = *(const bf16x8*)(b + t * 4096 + 2048), v1 = *(const bf16x8*)(b + t * 4096 + 3072);
      f32x16 s = qk(k0, k1, q0, q1, st.nv);
      if (t == 0 && ch == 0) attn_tile<true>(st, s, v0, v1); else attn_tile<false>(st, s, v0, v1);
    }
    if (ch + 1 < NCH) { char* d = lh + ((ch + 1) & 1) * (CH * 4096) + t9 * 16; *(u32x4*)d = sr[0]; *(u32x4*)(d + 8192) = sr[1]; }
    __syncthreads();
  }
  float* cb = (float*)lds + (wid & 7) * (17 * 64) + lane;
  if (half) {
#pragma unroll
    for (int i = 0; i < 16; ++i) cb[i * 64] = st.o[i];
    cb[16 * 64] = st.negm;
  }
  __syncthreads();
  if (!half) {
    const float nb = cb[16 * 64];
    const float nm = fminf(st.negm, nb);
    const float fa = __builtin_amdgcn_exp2f(nm - st.negm), fb = __builtin_amdgcn_exp2f(nm - nb);
#pragma unroll
    for (int i = 0; i < 16; ++i) st.o[i] = st.o[i] * fa + cb[i * 64] * fb;
    attn_store(st, O + (size_t)tok * 640 + c * 20, lane);
  }
}

__global__ __launch_bounds__(512) void attnB_k(const u16* __restrict__ Qp, const u16* __restrict__ KV, u16* __restrict__ O) {
  const int tid = threadIdx.x, wid = tid >> 6, lane = tid & 63;
  const int gw = blockIdx.x * 8 + wid, head = gw & 31, qt = gw >> 5;
  const int c = lane & 31, hi = lane >> 5, tok = qt * 32 + c;
  const u16* qp = Qp + (size_t)tok * 1024 + head * 32 + hi * 8;
  const bf16x8 q0 = *(const bf16x8*)qp, q1 = *(const bf16x8*)(qp + 16);
  AttnState st; st.o = f32x16{}; st.nv = f32x16{}; st.negm = 0.f;
  const u16* kvb = KV + (size_t)head * 64 * 2048 + lane * 8;
  const int tlo = qt >= 8 ? qt - 8 : 0;
  bf16x8 nk0, nk1, nv0, nv1;
  { const u16* b = kvb + qt * 2048; nk0 = *(const bf16x8*)b; nk1 = *(const bf16x8*)(b + 512); nv0 = *(const bf16x8*)(b + 1024); nv1 = *(const bf16x8*)(b + 1536); }
  for (int T = qt; T >= tlo; --T) {
    const bf16x8 k0 = nk0, k1 = nk1, v0 = nv0, v1 = nv1;
    if (T > tlo) { const u16* b = kvb + (T - 1) * 2048; nk0 = *(const bf16x8*)b; nk1 = *(const bf16x8*)(b + 512); nv0 = *(const bf16x8*)(b + 1024); nv1 = *(const bf16x8*)(b + 1536); }
    f32x16 s = qk(k0, k1, q0, q1, st.nv);
    if (T == qt) {
#pragma unroll
      for (int i = 0; i < 16; ++i) { const int row = (i & 3) + 8 * (i >> 2) + 4 * hi; if (row > c) s[i] = -1e30f; }
      attn_tile<true>(st, s, v0, v1);
    } else {
      if (T == qt - 8) {
#pragma unroll
        for (int i = 0; i < 16; ++i) { const int row = (i & 3) + 8 * (i >> 2) + 4 * hi; if (row <= c) s[i] = -1e30f; }
      }
      attn_tile<false>(st, s, v0, v1);
    }
  }
  attn_store(st, O + (size_t)tok * 640 + head * 20, lane);
}

extern "C" void kernel_launch(void* const* d_in, const int* in_sizes, int n_in, void* d_out, int out_size,
                              void* d_ws, size_t ws_size, hipStream_t stream) {
  const u16* x = (const u16*)d_in[0];
  const u16 *wq = (const u16*)d_in[1], *wk = (const u16*)d_in[2], *wv = (const u16*)d_in[3], *woA = (const u16*)d_in[4];
  const u16 *wqkv = (const u16*)d_in[6], *woB = (const u16*)d_in[7];
  char* w = (char*)d_ws;
  u16* xb0 = (u16*)(w);
  u16* xb1 = (u16*)(w + (4u << 20));
  u16* Qp = (u16*)(w + (8u << 20));
  u16* KV = (u16*)(w + (16u << 20));
  u16* ob = (u16*)(w + (28u << 20));
  u16* WT = (u16*)(w + (32u << 20));
  const size_t FB = 40 * 64 * 8;
  prep_k<<<122 * 40 * 64 / 256, 256, 0, stream>>>(wq, wk, wv, woA, wqkv, woB, WT);
  const u16* xin = x;
  u16* outs[4] = {xb0, xb1, xb0, (u16*)d_out};
  for (int layer = 0; layer < 4; ++layer) {
    if ((layer & 1) == 0) {
      gemm_k<0><<<dim3(32, 6), 256, 0, stream>>>(xin, WT, nullptr, Qp, KV, 22);
      attnA_k<<<256, 1024, 0, stream>>>(Qp, KV, ob);
      gemm_k<2><<<dim3(32, 5), 256, 0, stream>>>(ob, WT + 22 * FB, xin, outs[layer], nullptr, 20);
    } else {
      gemm_k<1><<<dim3(32, 15), 256, 0, stream>>>(xin, WT + 42 * FB, nullptr, Qp, KV, 60);
      attnB_k<<<256, 512, 0, stream>>>(Qp, KV, ob);
      gemm_k<2><<<dim3(32, 5), 256, 0, stream>>>(ob, WT + 102 * FB, xin, outs[layer], nullptr, 20);
    }
    xin = outs[layer];
  }
}
```
